```python
import torch

def _softmax(x, axis=-1):
    return torch.softmax(x, dim=axis)
def _gelu(x):
    return torch.nn.functional.gelu(x, approximate="tanh")
def _silu(x):
    return torch.nn.functional.silu(x)
def _sigmoid(x):
    return torch.sigmoid(x)
def _layernorm(x, axis=-1, eps=1e-5):
    m = x.mean(dim=axis, keepdim=True)
    v = x.var(dim=axis, keepdim=True, unbiased=False)
    return (x - m) / torch.sqrt(v + eps)
def _scatter_add(tgt, idx, src, axis=0):
    return tgt.clone().index_add_(axis, idx, src)
def _segment_sum(src, seg, n):
    out = torch.zeros((n,) + src.shape[1:], dtype=src.dtype)
    return out.index_add_(0, seg, src)
def _pad(x, pw):
    flat = tuple(v for pair in reversed(pw) for v in pair)
    return torch.nn.functional.pad(x, flat)

kernel_name = 'sequential_preLN__grouped_kv_mqa__mixer_s_mlp__prompt'

def setup_inputs():

    _w = (lambda a: (lambda t: t.to(torch.bfloat16) if t.is_floating_point() else t)(torch.as_tensor(a)))
    def _A_setup():
        B, S, D, H = 1, 2048, 640, 32
        Dh = D // H
        return {
            "x": _w((lambda *s: torch.randn(*s, dtype=torch.bfloat16) * 0.05)(B, S, D)),
            "wq": _w((lambda *s: torch.randn(*s, dtype=torch.bfloat16) * 0.05)(D, H * Dh)),
            "wk": _w((lambda *s: torch.randn(*s, dtype=torch.bfloat16) * 0.05)(D, Dh)),
            "wv": _w((lambda *s: torch.randn(*s, dtype=torch.bfloat16) * 0.05)(D, Dh)),
            "wo": _w((lambda *s: torch.randn(*s, dtype=torch.bfloat16) * 0.05)(H * Dh, D)),
        }
    _A_in = _A_setup()

    def _B_setup():
        B, S, D, F = 1, 2048, 640, 1728
        return {
            "x":  _w((lambda *s: torch.randn(*s, dtype=torch.bfloat16) * 0.05)(B, S, D)),
            "w1": _w((lambda *s: torch.randn(*s, dtype=torch.bfloat16) * 3.2)(D, D) * (D ** -0.5)),
            "w2": _w((lambda *s: torch.randn(*s, dtype=torch.bfloat16) * 3.2)(D, D) * (D ** -0.5)),
            "w3": _w((lambda *s: torch.randn(*s, dtype=torch.bfloat16) * 3.2)(D, F) * (D ** -0.5)),
            "w4": _w((lambda *s: torch.randn(*s, dtype=torch.bfloat16) * 3.2)(F, D) * (F ** -0.5)),
        }
    _B_in = _B_setup()

    return {
        "A_x": _A_in["x"],
        "A_wq": _A_in["wq"],
        "A_wk": _A_in["wk"],
        "A_wv": _A_in["wv"],
        "A_wo": _A_in["wo"],
        "B_x": _B_in["x"],
        "B_w1": _B_in["w1"],
        "B_w2": _B_in["w2"],
        "B_w3": _B_in["w3"],
        "B_w4": _B_in["w4"],
    }

def reference(A_x, A_wq, A_wk, A_wv, A_wo, B_x, B_w1, B_w2, B_w3, B_w4):
    def _A_ref(x, wq, wk, wv, wo):
        B, S, D = x.shape
        H = 32
        Dh = D // H
        q = (lambda x, *d: x.permute(*d))((x @ wq).reshape(B, S, H, Dh), 0, 2, 1, 3)
        k = x @ wk
        v = x @ wv
        scores = torch.einsum("bhqd,bkd->bhqk", q, k) * (Dh ** -0.5)
        p = _softmax(scores, axis=-1)
        o = torch.einsum("bhqk,bkd->bhqd", p, v)
        return (lambda x, *d: x.permute(*d))(o, 0, 2, 1, 3).reshape(B, S, H * Dh) @ wo

    def _B_ref(x, w1, w2, w3, w4):
        B, S, D = x.shape
        h = _gelu(_layernorm(x, axis=-1) @ w1)
        c = D // 4
        g0 = _pad(h[:, :, 0*c:1*c], ((0, 0), (1, 0), (0, 0)))[:, :S, :]
        g1 = _pad(h[:, :, 1*c:2*c], ((0, 0), (0, 1), (0, 0)))[:, 1:, :]
        g2 = _pad(h[:, :, 2*c:3*c], ((0, 0), (2, 0), (0, 0)))[:, :S, :]
        g3 = _pad(h[:, :, 3*c:   ], ((0, 0), (0, 2), (0, 0)))[:, 2:, :]
        hs = torch.concatenate([g0, g1, g2, g3], axis=-1) @ w2
        x1 = x + hs
        h2 = _gelu(_layernorm(x1, axis=-1) @ w3) @ w4
        return x1 + h2

    x = A_x
    h = x + _A_ref(_layernorm(x, axis=-1), A_wq, A_wk, A_wv, A_wo)
    h2 = h + _B_ref(_layernorm(h, axis=-1), B_w1, B_w2, B_w3, B_w4)
    return h2
```

```cpp
#include <hip/hip_runtime.h>
#include <hip/hip_bf16.h>
#include <stdint.h>
#include <type_traits>

typedef __attribute__((ext_vector_type(8))) __bf16 bf16x8;
typedef __attribute__((ext_vector_type(4))) __bf16 bf16x4;
typedef __attribute__((ext_vector_type(2))) __bf16 bf16x2;
typedef __attribute__((ext_vector_type(4))) short s16x4;
typedef __attribute__((ext_vector_type(8))) short s16x8;
typedef __attribute__((ext_vector_type(4))) float f32x4;
typedef __attribute__((ext_vector_type(16))) float f32x16;
typedef __attribute__((ext_vector_type(4))) unsigned u32x4;
typedef __attribute__((ext_vector_type(2))) unsigned u32x2;
typedef unsigned short u16;

#define DI __device__ __forceinline__
#define LDS_AS __attribute__((address_space(3)))

constexpr int S_LEN = 2048;
constexpr int DM = 640;
constexpr int FF = 1728;
constexpr int NH = 32;
constexpr int DH = 20;
constexpr float QSCALE = 0.22360679774997896f * 1.4426950408889634f;

constexpr int REC_KA = 0, REC_KB = 1024, REC_V = 1536, REC_B = 2880;

DI float bf2f(u16 v) { return __uint_as_float(((unsigned)v) << 16); }
DI float bfr(float f) { return (float)(__bf16)f; }
DI u16 f2bf(float f) { __bf16 b = (__bf16)f; return __builtin_bit_cast(u16, b); }
DI unsigned pack2(float lo, float hi) {
  bf16x2 p; p[0] = (__bf16)lo; p[1] = (__bf16)hi;
  return __builtin_bit_cast(unsigned, p);
}
DI float gelu_tanh(float x) {
  const float u = 0.7978845608028654f * (x + 0.044715f * x * x * x);
  const float e = __builtin_amdgcn_exp2f(-2.0f * 1.4426950408889634f * u);
  return x * __builtin_amdgcn_rcpf(1.0f + e);
}
DI bf16x8 tr_frag(const char* p0, const char* p1) {
  s16x4 a = __builtin_amdgcn_ds_read_tr16_b64_v4i16((LDS_AS s16x4*)p0);
  s16x4 b = __builtin_amdgcn_ds_read_tr16_b64_v4i16((LDS_AS s16x4*)p1);
  s16x8 c = __builtin_shufflevector(a, b, 0, 1, 2, 3, 4, 5, 6, 7);
  return __builtin_bit_cast(bf16x8, c);
}
DI void glds16(const void* g, void* lds_wave_base) {
  __builtin_amdgcn_global_load_lds((const unsigned*)g, (unsigned*)lds_wave_base, 16, 0, 0);
}

template <int KD> DI size_t taddr4(int row, int col) {
  return ((size_t)((row >> 4) * (KD / 32) + (col >> 5)) * 64 + ((col >> 3) & 3) * 16 + (row & 15)) * 8 +
         ((col & 4) ^ (((col >> 3) & 1) << 2));
}

DI void ln_stats(float sum, float sumsq, float& mean, float& var, float& m_bf, float& s_bf) {
  mean = sum * (1.0f / DM);
  var = fmaxf(sumsq * (1.0f / DM) - mean * mean, 0.0f);
  m_bf = bfr(mean);
  const float v_bf = bfr(var);
  s_bf = bfr(__builtin_sqrtf(bfr(v_bf + 1e-5f)));
}

enum { PRO_PLAIN = 0, PRO_LN = 1, PRO_LNLN = 2, PRO_SHIFT = 3 };
enum { EPI_QKV = 0, EPI_WO = 1, EPI_W1 = 2, EPI_W2 = 3, EPI_W3 = 4 };

DI void glds16_asm(const void* g, unsigned lds_base) {
  unsigned sv;
  asm volatile("s_mov_b32 %0, m0\n\ts_mov_b32 m0, %2\n\ts_nop 0\n\tglobal_load_lds_dwordx4 %1, off\n\ts_mov_b32 m0, %0"
               : "=&s"(sv) : "v"(g), "s"(lds_base) : "memory");
}
template <int OFF> DI u32x4 gload16_asm(const void* p) {
  u32x4 r;
  asm volatile("global_load_dwordx4 %0, %1, off offset:%2" : "=v"(r) : "v"(p), "i"(OFF) : "memory");
  return r;
}
template <int N> DI void wait_vm_bar() {
  asm volatile("s_waitcnt vmcnt(%0) lgkmcnt(0)\n\ts_barrier" ::"i"(N) : "memory");
}

template <int B, int E, class F> DI void static_for(F&& f) {
  if constexpr (B < E) { f(std::integral_constant<int, B>{}); static_for<B + 1, E>(f); }
}
template <int NT, int NTL, int RB, int PRO, int EPI>
__global__ __launch_bounds__(512) void gemm640_kernel(
    const u16* __restrict__ A,
    const u16* __restrict__ W,
    const u16* __restrict__ Wk,
    const u16* __restrict__ Wv,
    const u16* __restrict__ R,
    u16* __restrict__ Out,
    char* __restrict__ kvrec,
    float* __restrict__ aux,
    const float2* __restrict__ stat_in,
    float2* __restrict__ stat_out,
    int ldw, int ncols)
{
  asm volatile("" ::"s"(A), "s"(W), "s"(Wk), "s"(Wv), "s"(R), "s"(Out), "s"(kvrec), "s"(aux), "s"(stat_in), "s"(stat_out), "s"(ldw), "s"(ncols));
  constexpr int KS = 20, KH = 10;
  constexpr int RS = NTL * 32;
  constexpr int WBYTES = 640 * RS;
  constexpr int NCHUNK = 5;
  constexpr int PD = 3;
  constexpr int OPS = 2 * RB + (2 * NTL) / 4;
  constexpr int NT0 = (NT + 1) / 2;
  constexpr bool HAS_LN = (PRO == PRO_LN || PRO == PRO_LNLN);
  constexpr bool MFMA_STATS = (EPI == EPI_QKV);
  __shared__ __attribute__((aligned(16))) char smem[WBYTES + 2 * NT * 64 + 64];
  float* cs_lds = (float*)(smem + WBYTES);
  float* red_lds = (float*)(smem + WBYTES + 2 * NT * 64);
  const unsigned lds0 = (unsigned)(uintptr_t)smem;

  const int tid = threadIdx.x, lane = tid & 63;
  const int wid = __builtin_amdgcn_readfirstlane(tid >> 6);
  const int st = wid & 3, kh = wid >> 2;
  const int xcd = blockIdx.x & 7, idx = blockIdx.x >> 3;
  int nblk, mblk;
  if (EPI == EPI_W3) { nblk = idx & 15; mblk = xcd * 2 + (idx >> 4); }
  else { nblk = idx & 7; mblk = xcd * 4 + (idx >> 3); }
  const int n0 = nblk * NT * 16;
  const int r16 = lane & 15, g = lane >> 4;
  const int rowb = mblk * (64 * RB) + st * (16 * RB);
  const int tbeg = kh ? NT0 : 0, tend = kh ? NT : NT0;

  u32x2 rv[NT0];
  float ssum[RB], ssq[RB];
#pragma unroll
  for (int rb = 0; rb < RB; ++rb) { ssum[rb] = 0.f; ssq[rb] = 0.f; }
  if (EPI == EPI_WO || EPI == EPI_W2) {
#pragma unroll
    for (int t = 0; t < NT0; ++t) {
      const int n = n0 + min(tbeg + t, NT - 1) * 16 + g * 4;
      rv[t] = (EPI == EPI_WO) ? *(const u32x2*)(R + (size_t)(rowb + r16) * DM + n)
                              : *(const u32x2*)(R + taddr4<DM>(rowb + r16, n));
    }
  }
  if ((HAS_LN && !MFMA_STATS) || EPI == EPI_W2) {
#pragma unroll
    for (int rb = 0; rb < RB; ++rb) {
      const f32x4* sp = (const f32x4*)(stat_in + (size_t)(rowb + rb * 16 + r16) * 16 + g * 4);
      const f32x4 a = sp[0], b = sp[1];
      ssum[rb] = (a[0] + a[2]) + (b[0] + b[2]);
      ssq[rb] = (a[1] + a[3]) + (b[1] + b[3]);
    }
  }

  u32x4 afr[RB][KH];
  bool okr[RB][2];
  const u16* ap[RB][2];
#pragma unroll
  for (int rb = 0; rb < RB; ++rb) {
    const int row = rowb + rb * 16 + r16;
    if (PRO == PRO_SHIFT) {
#pragma unroll
      for (int j = 0; j < 2; ++j) {
        const int d = kh ? (j ? 2 : -2) : (j ? 1 : -1);
        const int rr = row + d;
        okr[rb][j] = (rr >= 0) && (rr < S_LEN);
        const int rc = okr[rb][j] ? rr : row;
        ap[rb][j] = A + ((size_t)((rc >> 4) * KS + kh * KH) * 64 + g * 16 + (rc & 15)) * 8;
      }
    } else if (EPI == EPI_QKV) {
      ap[rb][0] = A + (size_t)row * DM + kh * 320 + g * 8;
    } else {
      ap[rb][0] = A + ((size_t)(((rowb >> 4) + rb) * KS + kh * KH) * 64 + lane) * 8;
    }
  }
  int ntq = NT;
  if (EPI == EPI_QKV) ntq = (nblk < 6) ? 6 : (nblk == 6 ? 4 : 0);
  const char* wsrc = (const char*)W + (size_t)nblk * WBYTES + lane * 16;
  auto issue = [&](auto CH) {
    constexpr int ch = decltype(CH)::value;
    static_for<0, 2>([&](auto SS) {
      constexpr int s = ch * 2 + decltype(SS)::value;
      static_for<0, RB>([&](auto RR) {
        constexpr int rb = decltype(RR)::value;
        if constexpr (PRO == PRO_SHIFT) afr[rb][s] = gload16_asm<0>(ap[rb][s / 5] + s * 512);
        else if constexpr (EPI == EPI_QKV) afr[rb][s] = gload16_asm<s * 64>(ap[rb][0]);
        else afr[rb][s] = gload16_asm<(s & 3) * 1024>(ap[rb][0] + (s >> 2) * 2048);
      });
    });
    const int i0 = (kh * KH + ch * 2) * NTL;
    for (int i = i0 + st; i < i0 + 2 * NTL; i += 4) {
      if (EPI == EPI_QKV) {
        const int c = i * 64 + lane;
        const int krow = c / (2 * NTL), cc = c - krow * (2 * NTL);
        if (cc < 2 * ntq) glds16_asm(W + (size_t)krow * ldw + n0 + cc * 8, lds0 + i * 1024);
      } else {
        glds16_asm(wsrc + i * 1024, lds0 + i * 1024);
      }
    }
  };
  static_for<0, PD>(issue);
  bool drain_all = false;
  if (EPI == EPI_QKV) {
    if (nblk >= 6) {
      const u16* Wx = (nblk == 6) ? Wk : Wv;
      const int coff = (nblk == 6) ? 128 : 0;
      unsigned kv[20];
#pragma unroll
      for (int j = 0; j < 20; ++j) {
        const int i = tid + j * 512, k = i >> 4, dw = i & 15;
        kv[j] = (dw < 10) ? *(const unsigned*)(Wx + k * 20 + dw * 2) : 0u;
      }
#pragma unroll
      for (int j = 0; j < 20; ++j) {
        const int i = tid + j * 512, k = i >> 4, dw = i & 15;
        *(unsigned*)(smem + k * RS + coff + dw * 4) = kv[j];
      }
      drain_all = (nblk == 7);
    }
  }

  f32x4 acc[RB][NT];
#pragma unroll
  for (int rb = 0; rb < RB; ++rb)
#pragma unroll
    for (int t = 0; t < NT; ++t) acc[rb][t] = f32x4{0.f, 0.f, 0.f, 0.f};
  f32x4 sacc = {0.f, 0.f, 0.f, 0.f}, gacc = {0.f, 0.f, 0.f, 0.f};
  f32x4 cacc0 = {0.f, 0.f, 0.f, 0.f}, cacc1 = {0.f, 0.f, 0.f, 0.f};
  bf16x8 ones;
#pragma unroll
  for (int j = 0; j < 8; ++j) ones[j] = (__bf16)1.0f;
  const int godd = g & 1;
  const char* wb0 = smem + (kh * 320 + 8 * g + 4 * godd + (r16 >> 2)) * RS + (lane & 3) * 8;
  const char* wb1 = smem + (kh * 320 + 8 * g + 4 * (1 - godd) + (r16 >> 2)) * RS + (lane & 3) * 8;
  const int tc0 = st * 32, tc1 = min(st + 4, NT - 1) * 32;
  static_for<0, NCHUNK>([&](auto CH) {
    constexpr int ch = decltype(CH)::value;
    constexpr int lastc = (ch + PD - 1 < NCHUNK - 1) ? ch + PD - 1 : NCHUNK - 1;
    if (EPI == EPI_QKV && drain_all) asm volatile("s_waitcnt vmcnt(0)" ::: "memory");
    wait_vm_bar<(lastc - ch) * OPS>();
    static_for<0, 2>([&](auto SS) {
      constexpr int s = ch * 2 + decltype(SS)::value;
      static_for<0, RB>([&](auto RR) {
        constexpr int rb = decltype(RR)::value;
        asm volatile("" : "+v"(afr[rb][s]));
        u32x4 v = afr[rb][s];
        if (EPI == EPI_QKV && godd) v = u32x4{v[2], v[3], v[0], v[1]};
        if (PRO == PRO_SHIFT && !okr[rb][s / 5]) v = u32x4{0u, 0u, 0u, 0u};
        afr[rb][s] = v;
      });
    });
    if constexpr (ch + PD < NCHUNK) issue(std::integral_constant<int, ch + PD>{});
#pragma unroll
    for (int s = ch * 2; s < ch * 2 + 2; ++s) {
#pragma unroll
      for (int t = 0; t < NT; ++t) {
        const bf16x8 wf = tr_frag(wb0 + s * 32 * RS + t * 32, wb1 + s * 32 * RS + t * 32);
#pragma unroll
        for (int rb = 0; rb < RB; ++rb)
          acc[rb][t] = __builtin_amdgcn_mfma_f32_16x16x32_bf16(wf, __builtin_bit_cast(bf16x8, afr[rb][s]), acc[rb][t], 0, 0, 0);
      }
      if (HAS_LN) {
        const bf16x8 wc0 = tr_frag(wb0 + tc0 + s * 32 * RS, wb1 + tc0 + s * 32 * RS);
        cacc0 = __builtin_amdgcn_mfma_f32_16x16x32_bf16(wc0, ones, cacc0, 0, 0, 0);
        const bf16x8 wc1 = tr_frag(wb0 + tc1 + s * 32 * RS, wb1 + tc1 + s * 32 * RS);
        cacc1 = __builtin_amdgcn_mfma_f32_16x16x32_bf16(wc1, ones, cacc1, 0, 0, 0);
      }
      if (MFMA_STATS) {
        const bf16x8 af = __builtin_bit_cast(bf16x8, afr[0][s]);
        sacc = __builtin_amdgcn_mfma_f32_16x16x32_bf16(ones, af, sacc, 0, 0, 0);
        gacc = __builtin_amdgcn_mfma_f32_16x16x32_bf16(af, af, gacc, 0, 0, 0);
      }
    }
  });

  __syncthreads();
  {
    f32x4* xb = (f32x4*)smem;
    constexpr int XS = RB * NT0 + 1;
    const int obeg = kh ? 0 : NT0, oend = kh ? NT0 : NT;
#pragma unroll
    for (int rb = 0; rb < RB; ++rb)
#pragma unroll
      for (int t = 0; t < NT; ++t)
        if (t >= obeg && t < oend) xb[(wid * XS + rb * NT0 + (t - obeg)) * 64 + lane] = acc[rb][t];
    if (MFMA_STATS) {
      const int q3 = r16 & 3;
      const float dsel = (q3 == 0) ? gacc[0] : (q3 == 1) ? gacc[1] : (q3 == 2) ? gacc[2] : gacc[3];
      const float sq = __shfl(dsel, r16 + 16 * (r16 >> 2));
      xb[(wid * XS + RB * NT0) * 64 + lane] = f32x4{sacc[0], sq, 0.f, 0.f};
      ssum[0] = sacc[0]; ssq[0] = sq;
    }
    if (HAS_LN && r16 == 0) {
      *(f32x4*)(cs_lds + (kh * NT + st) * 16 + g * 4) = cacc0;
      if (st + 4 < NT) *(f32x4*)(cs_lds + (kh * NT + st + 4) * 16 + g * 4) = cacc1;
    }
    __syncthreads();
    const int pw = wid ^ 4;
#pragma unroll
    for (int rb = 0; rb < RB; ++rb)
#pragma unroll
      for (int t = 0; t < NT; ++t)
        if (t >= tbeg && t < tend) acc[rb][t] += xb[(pw * XS + rb * NT0 + (t - tbeg)) * 64 + lane];
    if (MFMA_STATS) {
      const f32x4 o = xb[(pw * XS + RB * NT0) * 64 + lane];
      ssum[0] += o[0]; ssq[0] += o[1];
    }
  }

  float la[RB], lb[RB], m_h[RB], sinv_h[RB];
#pragma unroll
  for (int rb = 0; rb < RB; ++rb) {
    la[rb] = 1.f; lb[rb] = 0.f; m_h[rb] = 0.f; sinv_h[rb] = 1.f;
    if (HAS_LN || EPI == EPI_W2) {
      float sum = ssum[rb], sq = ssq[rb];
      if (!MFMA_STATS) {
        sum += __shfl_xor(sum, 16); sum += __shfl_xor(sum, 32);
        sq += __shfl_xor(sq, 16); sq += __shfl_xor(sq, 32);
      }
      float mean, var, m_bf, s_bf;
      ln_stats(sum, sq, mean, var, m_bf, s_bf);
      const float inv_s = 1.0f / s_bf;
      m_h[rb] = m_bf; sinv_h[rb] = inv_s;
      if (PRO == PRO_LN) {
        la[rb] = inv_s; lb[rb] = -m_bf * inv_s;
      } else if (PRO == PRO_LNLN) {
        const float mean2 = (mean - m_bf) * inv_s;
        const float var2 = var * inv_s * inv_s;
        const float m2_bf = bfr(mean2);
        const float s2_bf = bfr(__builtin_sqrtf(bfr(bfr(var2) + 1e-5f)));
        const float inv_s2 = 1.0f / s2_bf;
        la[rb] = inv_s * inv_s2;
        lb[rb] = -(m_bf * inv_s + m2_bf) * inv_s2;
      }
    }
  }

  if (EPI == EPI_QKV) {
    const int row = rowb + r16;
    float vt[NT0][4];
#pragma unroll
    for (int t = 0; t < NT0; ++t) {
      const int tt = tbeg + t;
      const f32x4 c0 = *(const f32x4*)(cs_lds + tt * 16 + g * 4), c1 = *(const f32x4*)(cs_lds + (NT + tt) * 16 + g * 4);
#pragma unroll
      for (int r = 0; r < 4; ++r) vt[t][r] = la[0] * (kh ? acc[0][NT0 + t][r] : acc[0][t][r]) + lb[0] * (c0[r] + c1[r]);
      if (tt < ntq) {
        u32x2 o = {pack2(vt[t][0] * QSCALE, vt[t][1] * QSCALE), pack2(vt[t][2] * QSCALE, vt[t][3] * QSCALE)};
        *(u32x2*)(Out + (size_t)row * DM + n0 + tt * 16 + g * 4) = o;
      }
    }
    char* rec = kvrec + (size_t)(row >> 5) * REC_B;
    const int kk = row & 31;
    float n2 = 0.f;
    if (nblk == 6 && kh == 1) {
      float v0[4], v1[4];
#pragma unroll
      for (int r = 0; r < 4; ++r) { v0[r] = bfr(vt[1][r]); v1[r] = bfr(vt[2][r]); }
      u32x2 o = {pack2(v0[0], v0[1]), pack2(v0[2], v0[3])};
      *(u32x2*)(rec + REC_KA + (kk + 32 * (g >> 1)) * 16 + (g & 1) * 8) = o;
      n2 = v0[0] * v0[0] + v0[1] * v0[1] + v0[2] * v0[2] + v0[3] * v0[3];
      if (g == 0) {
        u32x4 o1 = {pack2(v1[0], v1[1]), pack2(v1[2], v1[3]), 0x00003F80u, 0u};
        *(u32x4*)(rec + REC_KB + kk * 16) = o1;
        n2 += v1[0] * v1[0] + v1[1] * v1[1] + v1[2] * v1[2] + v1[3] * v1[3];
      }
      n2 += __shfl_xor(n2, 16);
      n2 += __shfl_xor(n2, 32);
#pragma unroll
      for (int m = 1; m < 16; m <<= 1) n2 = fmaxf(n2, __shfl_xor(n2, m));
      if (lane == 0) red_lds[st] = n2;
    }
    if (nblk == 7 && kh == 0) {
      const int sst = kk >> 4, rem = kk & 15;
      const int hh = (rem >> 2) & 1, jj = 4 * (rem >> 3) + (rem & 3);
      u16* vb = (u16*)(rec + REC_V + ((sst * 2 + hh) * 21) * 16) + jj;
#pragma unroll
      for (int r = 0; r < 4; ++r) vb[(g * 4 + r) * 8] = f2bf(vt[0][r]);
      if (g == 0) {
#pragma unroll
        for (int r = 0; r < 4; ++r) vb[(16 + r) * 8] = f2bf(vt[1][r]);
      }
      if (g == 1) vb[20 * 8] = (u16)0x3F80;
    }
    if (nblk == 6) {
      __syncthreads();
      if (tid == 0) aux[mblk] = fmaxf(fmaxf(red_lds[0], red_lds[1]), fmaxf(red_lds[2], red_lds[3]));
    }
  } else {
#pragma unroll
    for (int rb = 0; rb < RB; ++rb) {
      const int row = rowb + rb * 16 + r16;
      float psum = 0.f, psq = 0.f;
#pragma unroll
      for (int t = 0; t < NT0; ++t) {
        const int tt = tbeg + t;
        if (tt >= tend) continue;
        const int n = n0 + tt * 16 + g * 4;
        if (EPI == EPI_W3 && n >= ncols) continue;
        float v[4];
#pragma unroll
        for (int r = 0; r < 4; ++r) v[r] = kh ? acc[rb][(NT0 + t < NT) ? NT0 + t : NT - 1][r] : acc[rb][t][r];
        if (HAS_LN) {
          const f32x4 c0 = *(const f32x4*)(cs_lds + tt * 16 + g * 4), c1 = *(const f32x4*)(cs_lds + (NT + tt) * 16 + g * 4);
#pragma unroll
          for (int r = 0; r < 4; ++r) v[r] = la[rb] * v[r] + lb[rb] * (c0[r] + c1[r]);
        }
        if (EPI == EPI_WO || EPI == EPI_W2) {
          float rr[4] = {__uint_as_float(rv[t][0] << 16), __uint_as_float(rv[t][0] & 0xffff0000u),
                         __uint_as_float(rv[t][1] << 16), __uint_as_float(rv[t][1] & 0xffff0000u)};
          if (EPI == EPI_W2) {
#pragma unroll
            for (int r = 0; r < 4; ++r) rr[r] = bfr(bfr(rr[r] - m_h[rb]) * sinv_h[rb]);
          }
#pragma unroll
          for (int r = 0; r < 4; ++r) {
            v[r] = bfr(rr[r] + bfr(v[r]));
            psum += v[r]; psq += v[r] * v[r];
          }
        } else {
#pragma unroll
          for (int r = 0; r < 4; ++r) v[r] = gelu_tanh(bfr(v[r]));
        }
        u32x2 o = {pack2(v[0], v[1]), pack2(v[2], v[3])};
        *(u32x2*)(Out + ((EPI == EPI_W3) ? taddr4<FF>(row, n) : taddr4<DM>(row, n))) = o;
      }
      if (EPI == EPI_WO || EPI == EPI_W2) {
        psum += __shfl_xor(psum, 16); psum += __shfl_xor(psum, 32);
        psq += __shfl_xor(psq, 16); psq += __shfl_xor(psq, 32);
        if (g == 0) stat_out[(size_t)row * 16 + nblk * 2 + kh] = make_float2(psum, psq);
      }
    }
  }
}

constexpr int WT_C640 = 8 * 640 * 10;
constexpr int WT_C3 = 16 * 640 * 14;
constexpr int WT_C4 = 8 * 1728 * 10;
constexpr int WT_TOTAL = 3 * WT_C640 + WT_C3 + WT_C4;
typedef __attribute__((address_space(1))) unsigned gu32;
typedef __attribute__((address_space(1))) unsigned long long gu64;
#define RLX_AGENT __ATOMIC_RELAXED, __HIP_MEMORY_SCOPE_AGENT
DI void st8_sc1(void* p, u32x2 v) {
  __hip_atomic_store((gu64*)p, ((unsigned long long)v[1] << 32) | v[0], RLX_AGENT);
}
DI u32x2 ld8_sc1(const void* p) {
  const unsigned long long x = __hip_atomic_load((gu64*)p, RLX_AGENT);
  return u32x2{(unsigned)x, (unsigned)(x >> 32)};
}
template <int OFF> DI u32x4 gload16_sc1(const void* p) {
  u32x4 r;
  asm volatile("global_load_dwordx4 %0, %1, off offset:%2 sc1" : "=v"(r) : "v"(p), "i"(OFF) : "memory");
  return r;
}
DI void phase_signal(unsigned* cnt) {
  asm volatile("s_waitcnt vmcnt(0) lgkmcnt(0)\n\ts_barrier" ::: "memory");
  if (threadIdx.x == 0) __hip_atomic_fetch_add((gu32*)cnt, 1u, RLX_AGENT);
}
DI void phase_wait(const unsigned* c0, const unsigned* c1, const unsigned* c2, unsigned want) {
  if (threadIdx.x < 64) {
    for (int spins = 0; spins < (1 << 16); ++spins) {
      const unsigned a = __hip_atomic_load((gu32*)c0, RLX_AGENT);
      const unsigned b = __hip_atomic_load((gu32*)c1, RLX_AGENT);
      const unsigned c = __hip_atomic_load((gu32*)c2, RLX_AGENT);
      if (__builtin_amdgcn_readfirstlane((a >= want && b >= want && c >= want) ? 1 : 0)) break;
      __builtin_amdgcn_s_sleep(1);
    }
  }
  asm volatile("s_waitcnt lgkmcnt(0)\n\ts_barrier" ::: "memory");
}

constexpr int PK_AUX = 640 * 224;
constexpr int PK_LDS = PK_AUX + 2 * 7 * 64;

template <int NTL> DI void w_issue640(const char* wimg, unsigned lds0, int wid, int lane) {
  for (int i = wid; i < 20 * NTL; i += 8) glds16_asm(wimg + (size_t)i * 1024 + lane * 16, lds0 + i * 1024);
}

template <int NT, int NTL, int RB, int PRO, int EPI>
DI void phase640(char* smem, const u16* A, const u16* R, u16* Out, const float2* stat_in, float2* stat_out,
                 int nblk, int mblk) {
  constexpr int KS = 20, KH = 10;
  constexpr int RS = NTL * 32;
  constexpr int NT0 = (NT + 1) / 2;
  constexpr bool HAS_LN = (PRO == PRO_LN || PRO == PRO_LNLN);
  float* cs_lds = (float*)(smem + PK_AUX);
  const int tid = threadIdx.x, lane = tid & 63;
  const int wid = __builtin_amdgcn_readfirstlane(tid >> 6);
  const int st = wid & 3, kh = wid >> 2;
  const int n0 = nblk * NT * 16;
  const int r16 = lane & 15, g = lane >> 4;
  const int rowb = mblk * (64 * RB) + st * (16 * RB);
  const int tbeg = kh ? NT0 : 0, tend = kh ? NT : NT0;

  u32x2 rv[NT0];
  u32x4 sraw[RB][2];
  if (EPI == EPI_WO || EPI == EPI_W2) {
#pragma unroll
    for (int t = 0; t < NT0; ++t) {
      const int n = n0 + min(tbeg + t, NT - 1) * 16 + g * 4;
      rv[t] = (EPI == EPI_WO) ? *(const u32x2*)(R + (size_t)(rowb + r16) * DM + n)
                              : ld8_sc1(R + taddr4<DM>(rowb + r16, n));
    }
  }
  u32x4 afr[RB][KH];
  bool okr[RB][2];
#pragma unroll
  for (int rb = 0; rb < RB; ++rb) {
    const int row = rowb + rb * 16 + r16;
    if (HAS_LN || EPI == EPI_W2) {
      const float2* sp = stat_in + (size_t)row * 16 + g * 4;
      sraw[rb][0] = gload16_sc1<0>(sp);
      sraw[rb][1] = gload16_sc1<16>(sp);
    }
  }
  static_for<0, KH>([&](auto SS) {
    constexpr int s = decltype(SS)::value;
    static_for<0, RB>([&](auto RR) {
      constexpr int rb = decltype(RR)::value;
      const int row = rowb + rb * 16 + r16;
      if constexpr (PRO == PRO_SHIFT) {
        const int d = kh ? ((s / 5) ? 2 : -2) : ((s / 5) ? 1 : -1);
        const int rr = row + d;
        okr[rb][s / 5] = (rr >= 0) && (rr < S_LEN);
        const int rc = okr[rb][s / 5] ? rr : row;
        afr[rb][s] = gload16_sc1<0>(A + ((size_t)((rc >> 4) * KS + kh * KH + s) * 64 + g * 16 + (rc & 15)) * 8);
      } else if constexpr (EPI == EPI_WO) {
        afr[rb][s] = gload16_asm<(s & 3) * 1024>(A + ((size_t)(((rowb >> 4) + rb) * KS + kh * KH + (s & ~3)) * 64 + lane) * 8);
      } else {
        afr[rb][s] = gload16_sc1<(s & 3) * 1024>(A + ((size_t)(((rowb >> 4) + rb) * KS + kh * KH + (s & ~3)) * 64 + lane) * 8);
      }
    });
  });

  f32x4 acc[RB][NT];
#pragma unroll
  for (int rb = 0; rb < RB; ++rb)
#pragma unroll
    for (int t = 0; t < NT; ++t) acc[rb][t] = f32x4{0.f, 0.f, 0.f, 0.f};
  f32x4 cacc0 = {0.f, 0.f, 0.f, 0.f}, cacc1 = {0.f, 0.f, 0.f, 0.f};
  bf16x8 ones;
#pragma unroll
  for (int j = 0; j < 8; ++j) ones[j] = (__bf16)1.0f;
  const int godd = g & 1;
  const char* wb0 = smem + (kh * 320 + 8 * g + 4 * godd + (r16 >> 2)) * RS + (lane & 3) * 8;
  const char* wb1 = smem + (kh * 320 + 8 * g + 4 * (1 - godd) + (r16 >> 2)) * RS + (lane & 3) * 8;
  const int tc0 = st * 32, tc1 = min(st + 4, NT - 1) * 32;
  static_for<0, 5>([&](auto CH) {
    constexpr int ch = decltype(CH)::value;
    if constexpr (ch == 0) wait_vm_bar<RB * (KH - 2)>();
    else asm volatile("s_waitcnt vmcnt(%0)" ::"i"(RB * (KH - 2 * (ch + 1))) : "memory");
#pragma unroll
    for (int s = ch * 2; s < ch * 2 + 2; ++s) {
#pragma unroll
      for (int rb = 0; rb < RB; ++rb) {
        u32x4& ar = afr[rb][s];
        asm volatile("" : "+v"(ar));
        if (PRO == PRO_SHIFT && !okr[rb][s / 5]) ar = u32x4{0u, 0u, 0u, 0u};
      }
    }
#pragma unroll
    for (int s = ch * 2; s < ch * 2 + 2; ++s) {
#pragma unroll
      for (int t = 0; t < NT; ++t) {
        const bf16x8 wf = tr_frag(wb0 + s * 32 * RS + t * 32, wb1 + s * 32 * RS + t * 32);
#pragma unroll
        for (int rb = 0; rb < RB; ++rb)
          acc[rb][t] = __builtin_amdgcn_mfma_f32_16x16x32_bf16(wf, __builtin_bit_cast(bf16x8, afr[rb][s]), acc[rb][t], 0, 0, 0);
      }
      if (HAS_LN) {
        const bf16x8 wc0 = tr_frag(wb0 + tc0 + s * 32 * RS, wb1 + tc0 + s * 32 * RS);
        cacc0 = __builtin_amdgcn_mfma_f32_16x16x32_bf16(wc0, ones, cacc0, 0, 0, 0);
        const bf16x8 wc1 = tr_frag(wb0 + tc1 + s * 32 * RS, wb1 + tc1 + s * 32 * RS);
        cacc1 = __builtin_amdgcn_mfma_f32_16x16x32_bf16(wc1, ones, cacc1, 0, 0, 0);
      }
    }
  });

  __syncthreads();
  {
    f32x4* xb = (f32x4*)smem;
    constexpr int XS = RB * NT0;
    const int obeg = kh ? 0 : NT0, oend = kh ? NT0 : NT;
#pragma unroll
    for (int rb = 0; rb < RB; ++rb)
#pragma unroll
      for (int t = 0; t < NT; ++t)
        if (t >= obeg && t < oend) xb[(wid * XS + rb * NT0 + (t - obeg)) * 64 + lane] = acc[rb][t];
    if (HAS_LN && r16 == 0) {
      *(f32x4*)(cs_lds + (kh * NT + st) * 16 + g * 4) = cacc0;
      if (st + 4 < NT) *(f32x4*)(cs_lds + (kh * NT + st + 4) * 16 + g * 4) = cacc1;
    }
    __syncthreads();
    const int pw = wid ^ 4;
#pragma unroll
    for (int rb = 0; rb < RB; ++rb)
#pragma unroll
      for (int t = 0; t < NT; ++t)
        if (t >= tbeg && t < tend) acc[rb][t] += xb[(pw * XS + rb * NT0 + (t - tbeg)) * 64 + lane];
  }

#pragma unroll
  for (int rb = 0; rb < RB; ++rb) {
    float la = 1.f, lb = 0.f, m_h = 0.f, sinv_h = 1.f;
    if (HAS_LN || EPI == EPI_W2) {
      asm volatile("" : "+v"(sraw[rb][0]), "+v"(sraw[rb][1]));
      const f32x4 a = __builtin_bit_cast(f32x4, sraw[rb][0]), b = __builtin_bit_cast(f32x4, sraw[rb][1]);
      float sum = (a[0] + a[2]) + (b[0] + b[2]), sq = (a[1] + a[3]) + (b[1] + b[3]);
      sum += __shfl_xor(sum, 16); sum += __shfl_xor(sum, 32);
      sq += __shfl_xor(sq, 16); sq += __shfl_xor(sq, 32);
      float mean, var, m_bf, s_bf;
      ln_stats(sum, sq, mean, var, m_bf, s_bf);
      const float inv_s = 1.0f / s_bf;
      m_h = m_bf; sinv_h = inv_s;
      if (PRO == PRO_LN) {
        la = inv_s; lb = -m_bf * inv_s;
      } else if (PRO == PRO_LNLN) {
        const float mean2 = (mean - m_bf) * inv_s;
        const float var2 = var * inv_s * inv_s;
        const float m2_bf = bfr(mean2);
        const float s2_bf = bfr(__builtin_sqrtf(bfr(bfr(var2) + 1e-5f)));
        const float inv_s2 = 1.0f / s2_bf;
        la = inv_s * inv_s2;
        lb = -(m_bf * inv_s + m2_bf) * inv_s2;
      }
    }
    const int row = rowb + rb * 16 + r16;
    float psum = 0.f, psq = 0.f;
#pragma unroll
    for (int t = 0; t < NT0; ++t) {
      const int tt = tbeg + t;
      if (tt >= tend) continue;
      const int n = n0 + tt * 16 + g * 4;
      if (EPI == EPI_W3 && n >= FF) continue;
      float v[4];
#pragma unroll
      for (int r = 0; r < 4; ++r) v[r] = kh ? acc[rb][(NT0 + t < NT) ? NT0 + t : NT - 1][r] : acc[rb][t][r];
      if (HAS_LN) {
        const f32x4 c0 = *(const f32x4*)(cs_lds + tt * 16 + g * 4), c1 = *(const f32x4*)(cs_lds + (NT + tt) * 16 + g * 4);
#pragma unroll
        for (int r = 0; r < 4; ++r) v[r] = la * v[r] + lb * (c0[r] + c1[r]);
      }
      if (EPI == EPI_WO || EPI == EPI_W2) {
        float rr[4] = {__uint_as_float(rv[t][0] << 16), __uint_as_float(rv[t][0] & 0xffff0000u),
                       __uint_as_float(rv[t][1] << 16), __uint_as_float(rv[t][1] & 0xffff0000u)};
        if (EPI == EPI_W2) {
#pragma unroll
          for (int r = 0; r < 4; ++r) rr[r] = bfr(bfr(rr[r] - m_h) * sinv_h);
        }
#pragma unroll
        for (int r = 0; r < 4; ++r) {
          v[r] = bfr(rr[r] + bfr(v[r]));
          psum += v[r]; psq += v[r] * v[r];
        }
      } else {
#pragma unroll
        for (int r = 0; r < 4; ++r) v[r] = gelu_tanh(bfr(v[r]));
      }
      const u32x2 o = {pack2(v[0], v[1]), pack2(v[2], v[3])};
      st8_sc1(Out + ((EPI == EPI_W3) ? taddr4<FF>(row, n) : taddr4<DM>(row, n)), o);
    }
    if (EPI == EPI_WO || EPI == EPI_W2) {
      psum += __shfl_xor(psum, 16); psum += __shfl_xor(psum, 32);
      psq += __shfl_xor(psq, 16); psq += __shfl_xor(psq, 32);
      if (g == 0) st8_sc1(stat_out + (size_t)row * 16 + nblk * 2 + kh, u32x2{__float_as_uint(psum), __float_as_uint(psq)});
    }
  }
}

__global__ __launch_bounds__(512) void post_kernel(const u16* __restrict__ O, const u16* __restrict__ X,
                                                   const char* __restrict__ wt, u16* H, u16* G1, u16* X1, u16* G,
                                                   float2* st_h, float2* st_x1, unsigned* cnt, u16* __restrict__ Out)
{
  asm volatile("" ::"s"(O), "s"(X), "s"(wt), "s"(H), "s"(G1), "s"(X1), "s"(G), "s"(st_h), "s"(st_x1), "s"(cnt), "s"(Out));
  __shared__ __attribute__((aligned(16))) char smem[PK_LDS];
  const unsigned lds0 = (unsigned)(uintptr_t)smem;
  const int tid = threadIdx.x, lane = tid & 63;
  const int wid = __builtin_amdgcn_readfirstlane(tid >> 6);
  const int xcd = blockIdx.x & 7, idx = blockIdx.x >> 3;
  const int nblk = idx & 7, mblk = xcd * 4 + (idx >> 3);
  const int nblk3 = idx & 15, mblk3 = xcd * 2 + (idx >> 4);
  const char* wo_t = wt;
  const char* w1_t = wt + (size_t)WT_C640 * 16;
  const char* w2_t = wt + (size_t)2 * WT_C640 * 16;
  const char* w3_t = wt + (size_t)3 * WT_C640 * 16;
  const char* w4_t = wt + (size_t)(3 * WT_C640 + WT_C3) * 16;
  unsigned* cA = cnt;
  unsigned* cB = cnt + 512;
  unsigned* cC = cnt + 1024;
  unsigned* cD = cnt + 1536;

  w_issue640<5>(wo_t + (size_t)nblk * (640 * 160), lds0, wid, lane);
  phase640<5, 5, 1, PRO_PLAIN, EPI_WO>(smem, O, X, H, nullptr, st_h, nblk, mblk);
  phase_signal(cA + mblk * 16);
  w_issue640<5>(w1_t + (size_t)nblk * (640 * 160), lds0, wid, lane);
  phase_wait(cA + mblk * 16, cA + mblk * 16, cA + mblk * 16, 8u);
  phase640<5, 5, 1, PRO_LNLN, EPI_W1>(smem, H, nullptr, G1, st_h, nullptr, nblk, mblk);
  phase_signal(cB + mblk * 16);
  w_issue640<5>(w2_t + (size_t)nblk * (640 * 160), lds0, wid, lane);
  phase_wait(cB + max(mblk - 1, 0) * 16, cB + mblk * 16, cB + min(mblk + 1, 31) * 16, 8u);
  phase640<5, 5, 1, PRO_SHIFT, EPI_W2>(smem, G1, H, X1, st_h, st_x1, nblk, mblk);
  phase_signal(cC + mblk * 16);
  w_issue640<7>(w3_t + (size_t)nblk3 * (640 * 224), lds0, wid, lane);
  phase_wait(cC + (2 * mblk3) * 16, cC + (2 * mblk3 + 1) * 16, cC + (2 * mblk3) * 16, 8u);
  phase640<7, 7, 2, PRO_LN, EPI_W3>(smem, X1, nullptr, G, st_x1, nullptr, nblk3, mblk3);
  phase_signal(cD + mblk3 * 16);

  {
    constexpr int NT = 5, NT0 = 3, RS = 160;
    constexpr int CK = 3, NCHUNK = 9, PD = 3, NSLOT = 4;
    constexpr int HB = CK * 32 * RS;
    constexpr int OPS = CK + 15 / 4;
    const int st = wid & 3, kh = wid >> 2;
    const int n0 = nblk * 80;
    const int r16 = lane & 15, g = lane >> 4;
    const int row = mblk * 64 + st * 16 + r16;
    const int tbeg = kh ? NT0 : 0, tend = kh ? NT : NT0;
    const char* wsrc = w4_t + (size_t)nblk * (FF * RS) + (size_t)kh * (27 * 32 * RS) + lane * 16;
    auto issue_w = [&](int ch) {
      for (int i = st; i < 15; i += 4)
        glds16_asm(wsrc + ch * HB + i * 1024, lds0 + ((ch % NSLOT) * 2 + kh) * HB + i * 1024);
    };
    for (int ch = 0; ch < PD; ++ch) issue_w(ch);
    phase_wait(cD + (mblk >> 1) * 16, cD + (mblk >> 1) * 16, cD + (mblk >> 1) * 16, 16u);
    u32x2 xv[NT0], hv[NT0];
#pragma unroll
    for (int t = 0; t < NT0; ++t) {
      const int n = n0 + min(tbeg + t, NT - 1) * 16 + g * 4;
      xv[t] = ld8_sc1(X1 + taddr4<DM>(row, n));
      hv[t] = ld8_sc1(H + taddr4<DM>(row, n));
    }
    const u16* gsrc = G + ((size_t)((mblk * 4 + st) * 54 + kh * 27) * 64 + lane) * 8;
    u32x4 afr[NSLOT][CK];
    auto issue_a = [&](auto CH) {
      constexpr int ch = decltype(CH)::value;
      static_for<0, CK>([&](auto SS) {
        constexpr int s = ch * CK + decltype(SS)::value;
        afr[ch % NSLOT][decltype(SS)::value] = gload16_sc1<(s & 3) * 1024>(gsrc + (s >> 2) * 2048);
      });
    };
    f32x4 acc[NT];
#pragma unroll
    for (int t = 0; t < NT; ++t) acc[t] = f32x4{0.f, 0.f, 0.f, 0.f};
    const int godd = g & 1;
    const char* wb0 = smem + kh * HB + (8 * g + 4 * godd + (r16 >> 2)) * RS + (lane & 3) * 8;
    const char* wb1 = smem + kh * HB + (8 * g + 4 * (1 - godd) + (r16 >> 2)) * RS + (lane & 3) * 8;
    static_for<0, PD>(issue_a);
    static_for<0, NCHUNK>([&](auto CH) {
      constexpr int ch = decltype(CH)::value;
      constexpr int lastc = (ch + PD - 1 < NCHUNK - 1) ? ch + PD - 1 : NCHUNK - 1;
      constexpr int younger = (ch == 0) ? 2 * CK : (ch == 1) ? CK + OPS : (lastc - ch) * OPS;
      wait_vm_bar<younger>();
      bf16x8 af[CK];
#pragma unroll
      for (int s = 0; s < CK; ++s) {
        u32x4& ar = afr[ch % NSLOT][s];
        asm volatile("" : "+v"(ar));
        af[s] = __builtin_bit_cast(bf16x8, ar);
      }
      if constexpr (ch + PD < NCHUNK) { issue_a(std::integral_constant<int, ch + PD>{}); issue_w(ch + PD); }
#pragma unroll
      for (int s = 0; s < CK; ++s) {
#pragma unroll
        for (int t = 0; t < NT; ++t) {
          const int off = (ch % NSLOT) * 2 * HB + s * 32 * RS + t * 32;
          const bf16x8 wf = tr_frag(wb0 + off, wb1 + off);
          acc[t] = __builtin_amdgcn_mfma_f32_16x16x32_bf16(wf, af[s], acc[t], 0, 0, 0);
        }
      }
    });
    __syncthreads();
    f32x4* xb = (f32x4*)smem;
    {
      const int obeg = kh ? 0 : NT0, oend = kh ? NT0 : NT;
#pragma unroll
      for (int t = 0; t < NT; ++t)
        if (t >= obeg && t < oend) xb[(wid * NT0 + (t - obeg)) * 64 + lane] = acc[t];
    }
    __syncthreads();
#pragma unroll
    for (int t = 0; t < NT0; ++t) {
      const int tt = tbeg + t;
      if (tt >= tend) continue;
      const f32x4 o = xb[((wid ^ 4) * NT0 + t) * 64 + lane];
      const f32x4 a = kh ? acc[(NT0 + t < NT) ? NT0 + t : NT - 1] : acc[t];
      const int n = n0 + tt * 16 + g * 4;
      const float xr[4] = {__uint_as_float(xv[t][0] << 16), __uint_as_float(xv[t][0] & 0xffff0000u),
                           __uint_as_float(xv[t][1] << 16), __uint_as_float(xv[t][1] & 0xffff0000u)};
      const float hr[4] = {__uint_as_float(hv[t][0] << 16), __uint_as_float(hv[t][0] & 0xffff0000u),
                           __uint_as_float(hv[t][1] << 16), __uint_as_float(hv[t][1] & 0xffff0000u)};
      float v[4];
#pragma unroll
      for (int r = 0; r < 4; ++r) v[r] = hr[r] + bfr(xr[r] + bfr(a[r] + o[r]));
      u32x2 ov = {pack2(v[0], v[1]), pack2(v[2], v[3])};
      *(u32x2*)(Out + (size_t)row * DM + n) = ov;
    }
  }
}

constexpr int AT_NSLOT = 56;
constexpr int AT_LDS = AT_NSLOT * REC_B + 512;

template <bool SUB>
DI void attn_tile(const char* sb, int lane, const bf16x8 (&q0)[2], const bf16x8 (&q1)[2], const float (&m)[2],
                  f32x16 (&o)[2]) {
  const int r = lane & 31, h = lane >> 5;
  const bf16x8 ka = *(const bf16x8*)(sb + REC_KA + lane * 16);
  const bf16x8 kb = *(const bf16x8*)(sb + REC_KB + r * 16);
  const bf16x8 v0 = *(const bf16x8*)(sb + REC_V + (h * 21 + r) * 16);
  const bf16x8 v1 = *(const bf16x8*)(sb + REC_V + ((2 + h) * 21 + r) * 16);
  f32x16 s[2];
#pragma unroll
  for (int qt = 0; qt < 2; ++qt) {
    s[qt] = __builtin_amdgcn_mfma_f32_32x32x16_bf16(ka, q0[qt], f32x16{}, 0, 0, 0);
    s[qt] = __builtin_amdgcn_mfma_f32_32x32x16_bf16(kb, q1[qt], s[qt], 0, 0, 0);
  }
#pragma unroll
  for (int qt = 0; qt < 2; ++qt) {
    bf16x8 p0, p1;
#pragma unroll
    for (int i = 0; i < 8; ++i) {
      p0[i] = (__bf16)__builtin_amdgcn_exp2f(SUB ? (s[qt][i] - m[qt]) : s[qt][i]);
      p1[i] = (__bf16)__builtin_amdgcn_exp2f(SUB ? (s[qt][8 + i] - m[qt]) : s[qt][8 + i]);
    }
    o[qt] = __builtin_amdgcn_mfma_f32_32x32x16_bf16(v0, p0, o[qt], 0, 0, 0);
    o[qt] = __builtin_amdgcn_mfma_f32_32x32x16_bf16(v1, p1, o[qt], 0, 0, 0);
  }
}

DI const u16* wt_src(int c, const u16* wo, const u16* w1, const u16* w2, const u16* w3, const u16* w4) {
  if (c < 3 * WT_C640) {
    const int m = c / WT_C640, r = c - m * WT_C640;
    const u16* w = (m == 0) ? wo : (m == 1) ? w1 : w2;
    const int nb = r / 6400, q = r - nb * 6400, k = q / 10, cc = q - k * 10;
    return w + (size_t)k * DM + nb * 80 + cc * 8;
  }
  c -= 3 * WT_C640;
  if (c < WT_C3) {
    const int nb = c / 8960, q = c - nb * 8960, k = q / 14, cc = q - k * 14;
    return w3 + (size_t)k * FF + min(nb * 112 + cc * 8, FF - 8);
  }
  c -= WT_C3;
  const int nb = c / 17280, q = c - nb * 17280, k = q / 10, cc = q - k * 10;
  return w4 + (size_t)k * DM + nb * 80 + cc * 8;
}

__global__ __launch_bounds__(1024) void attn_kernel(const u16* __restrict__ Q, const char* __restrict__ kvrec,
                                                    const float* __restrict__ kmaxp, u16* __restrict__ O,
                                                    const u16* __restrict__ wo, const u16* __restrict__ w1,
                                                    const u16* __restrict__ w2, const u16* __restrict__ w3,
                                                    const u16* __restrict__ w4, u32x4* __restrict__ wt,
                                                    unsigned* __restrict__ cnt)
{
  asm volatile("" ::"s"(Q), "s"(kvrec), "s"(kmaxp), "s"(O), "s"(wo), "s"(w1), "s"(w2), "s"(w3), "s"(w4), "s"(wt), "s"(cnt));
  __shared__ __attribute__((aligned(16))) char smem[AT_LDS];
  const int tid = threadIdx.x, lane = tid & 63;
  const int wid = __builtin_amdgcn_readfirstlane(tid >> 6);
  const int qg = wid & 3, kq = wid >> 2;
  const int c = lane & 31, h = lane >> 5;
  const int tok0 = blockIdx.x * 8 + qg * 2;
  const unsigned lds0 = (unsigned)(uintptr_t)smem;
  volatile int* flag = (volatile int*)(smem + AT_NSLOT * REC_B + 256);
  constexpr int RCH = 8 * REC_B / 16;
  constexpr int NCH7 = 56 * REC_B / 16;

  u32x2 qa[2], qb[2], qe[2];
  float km;
#pragma unroll
  for (int qt = 0; qt < 2; ++qt) {
    const u16* qp = Q + (size_t)(tok0 + qt) * DM + c * DH;
    asm volatile("global_load_dwordx2 %0, %1, off" : "=v"(qa[qt]) : "v"(qp + 8 * h) : "memory");
    asm volatile("global_load_dwordx2 %0, %1, off offset:8" : "=v"(qb[qt]) : "v"(qp + 8 * h) : "memory");
    asm volatile("global_load_dwordx2 %0, %1, off offset:32" : "=v"(qe[qt]) : "v"(qp) : "memory");
  }
  asm volatile("global_load_dword %0, %1, off" : "=v"(km) : "v"(kmaxp + c) : "memory");
  for (int i = wid; i * 64 < RCH; i += 16) {
    const int ch = i * 64 + lane;
    if (ch < RCH) glds16_asm(kvrec + (size_t)ch * 16, lds0 + i * 1024);
  }
  const int wc0 = blockIdx.x * 1024 + tid, wc1 = wc0 + 256 * 1024;
  const u32x4 wtv0 = *(const u32x4*)wt_src(wc0, wo, w1, w2, w3, w4);
  u32x4 wtv1 = {0u, 0u, 0u, 0u};
  if (wc1 < WT_TOTAL) wtv1 = *(const u32x4*)wt_src(wc1, wo, w1, w2, w3, w4);
  if (tid == 0) *flag = 0;
  asm volatile("s_waitcnt vmcnt(2)" ::: "memory");
  asm volatile("" : "+v"(qa[0]), "+v"(qa[1]), "+v"(qb[0]), "+v"(qb[1]), "+v"(qe[0]), "+v"(qe[1]), "+v"(km));
  bf16x8 q0[2], q1[2];
#pragma unroll
  for (int mm = 1; mm < 32; mm <<= 1) km = fmaxf(km, __shfl_xor(km, mm));
#pragma unroll
  for (int qt = 0; qt < 2; ++qt) {
    const u32x2 e = h ? u32x2{0u, 0u} : qe[qt];
    const u32x4 f0 = {qa[qt][0], qa[qt][1], qb[qt][0], qb[qt][1]};
    q0[qt] = __builtin_bit_cast(bf16x8, f0);
    float n2 = 0.f;
#pragma unroll
    for (int j = 0; j < 4; ++j) {
      const float lo = __uint_as_float(f0[j] << 16), hi = __uint_as_float(f0[j] & 0xffff0000u);
      n2 += lo * lo + hi * hi;
    }
#pragma unroll
    for (int j = 0; j < 2; ++j) {
      const float lo = __uint_as_float(e[j] << 16), hi = __uint_as_float(e[j] & 0xffff0000u);
      n2 += lo * lo + hi * hi;
    }
    n2 += __shfl_xor(n2, 32);
    const float mref = __builtin_sqrtf(n2 * km);
    const unsigned negm = h ? 0u : (unsigned)f2bf(-mref);
    const u32x4 f1 = {e[0], e[1], negm, 0u};
    q1[qt] = __builtin_bit_cast(bf16x8, f1);
  }
  asm volatile("s_waitcnt vmcnt(0) lgkmcnt(0)\n\ts_barrier" ::: "memory");
  for (int i = wid; i * 64 < NCH7 - RCH; i += 16) {
    const int ch = i * 64 + lane;
    if (ch < NCH7 - RCH) glds16_asm(kvrec + (size_t)(RCH + ch) * 16, lds0 + RCH * 16 + i * 1024);
  }

  f32x16 o[2] = {};
  const float mz[2] = {0.f, 0.f};
  attn_tile<false>(smem + (2 * kq) * REC_B, lane, q0, q1, mz, o);
  attn_tile<false>(smem + (2 * kq + 1) * REC_B, lane, q0, q1, mz, o);
  asm volatile("s_waitcnt vmcnt(0) lgkmcnt(0)\n\ts_barrier" ::: "memory");
  for (int i = wid; i * 64 < RCH; i += 16) {
    const int ch = i * 64 + lane;
    if (ch < RCH) glds16_asm(kvrec + (size_t)(56 * REC_B) + (size_t)ch * 16, lds0 + i * 1024);
  }
  if (blockIdx.x == 0) { cnt[tid] = 0u; cnt[tid + 1024] = 0u; }
  wt[wc0] = wtv0;
  if (wc1 < WT_TOTAL) wt[wc1] = wtv1;
  for (int r = 1; r < 7; ++r) {
    attn_tile<false>(smem + (8 * r + 2 * kq) * REC_B, lane, q0, q1, mz, o);
    attn_tile<false>(smem + (8 * r + 2 * kq + 1) * REC_B, lane, q0, q1, mz, o);
  }
  asm volatile("s_waitcnt vmcnt(0) lgkmcnt(0)\n\ts_barrier" ::: "memory");
  attn_tile<false>(smem + (2 * kq) * REC_B, lane, q0, q1, mz, o);
  attn_tile<false>(smem + (2 * kq + 1) * REC_B, lane, q0, q1, mz, o);

  f32x4* red = (f32x4*)smem;
  for (int pass = 0; pass < 2; ++pass) {
    __syncthreads();
#pragma unroll
    for (int qt = 0; qt < 2; ++qt)
#pragma unroll
      for (int qd = 0; qd < 3; ++qd)
        red[((kq * 8 + qg * 2 + qt) * 3 + qd) * 64 + lane] =
            f32x4{o[qt][4 * qd], o[qt][4 * qd + 1], o[qt][4 * qd + 2], o[qt][4 * qd + 3]};
    __syncthreads();
    bool bad = false;
    for (int it = wid; it < 24; it += 16) {
      const int qtile = it / 3, qd = it - qtile * 3;
      f32x4 a = red[((0 * 8 + qtile) * 3 + qd) * 64 + lane];
      float l = red[((0 * 8 + qtile) * 3 + 2) * 64 + c + 32][0];
#pragma unroll
      for (int k = 1; k < 4; ++k) {
        a += red[((k * 8 + qtile) * 3 + qd) * 64 + lane];
        l += red[((k * 8 + qtile) * 3 + 2) * 64 + c + 32][0];
      }
      bad |= !(l >= 9.313225746154785e-10f);
      const float inv = 1.0f / l;
      if (qd < 2 || h == 0) {
        u32x2 ov = {pack2(a[0] * inv, a[1] * inv), pack2(a[2] * inv, a[3] * inv)};
        *(u32x2*)(O + taddr4<DM>(blockIdx.x * 8 + qtile, c * DH + 4 * h + 8 * qd)) = ov;
      }
    }
    if (pass == 1) break;
    if (__any(bad) && lane == 0) *flag = 1;
    __syncthreads();
    if (!*flag) break;
    float mx[2] = {-INFINITY, -INFINITY};
    bf16x8 q1z[2];
#pragma unroll
    for (int qt = 0; qt < 2; ++qt) {
      u32x4 f = __builtin_bit_cast(u32x4, q1[qt]);
      f[2] = 0u;
      q1z[qt] = __builtin_bit_cast(bf16x8, f);
    }
    for (int r = 0; r < 8; ++r) {
      __syncthreads();
      for (int ch = tid; ch < RCH; ch += 1024)
        *(u32x4*)(smem + ch * 16) = *(const u32x4*)(kvrec + (size_t)r * 8 * REC_B + (size_t)ch * 16);
      __syncthreads();
      for (int tt = 0; tt < 2; ++tt) {
        const char* sb = smem + (2 * kq + tt) * REC_B;
        const bf16x8 ka = *(const bf16x8*)(sb + REC_KA + lane * 16);
        const bf16x8 kb = *(const bf16x8*)(sb + REC_KB + c * 16);
#pragma unroll
        for (int qt = 0; qt < 2; ++qt) {
          f32x16 s = {};
          s = __builtin_amdgcn_mfma_f32_32x32x16_bf16(ka, q0[qt], s, 0, 0, 0);
          s = __builtin_amdgcn_mfma_f32_32x32x16_bf16(kb, q1z[qt], s, 0, 0, 0);
#pragma unroll
          for (int i = 0; i < 16; ++i) mx[qt] = fmaxf(mx[qt], s[i]);
        }
      }
    }
    __syncthreads();
    float* mb = (float*)smem;
#pragma unroll
    for (int qt = 0; qt < 2; ++qt) {
      mx[qt] = fmaxf(mx[qt], __shfl_xor(mx[qt], 32));
      if (h == 0) mb[(kq * 8 + qg * 2 + qt) * 32 + c] = mx[qt];
    }
    __syncthreads();
    float mt[2];
#pragma unroll
    for (int qt = 0; qt < 2; ++qt) {
      float a = mb[(0 * 8 + qg * 2 + qt) * 32 + c];
#pragma unroll
      for (int k = 1; k < 4; ++k) a = fmaxf(a, mb[(k * 8 + qg * 2 + qt) * 32 + c]);
      mt[qt] = a;
    }
    o[0] = f32x16{}; o[1] = f32x16{};
    for (int r = 0; r < 8; ++r) {
      __syncthreads();
      for (int ch = tid; ch < RCH; ch += 1024)
        *(u32x4*)(smem + ch * 16) = *(const u32x4*)(kvrec + (size_t)r * 8 * REC_B + (size_t)ch * 16);
      __syncthreads();
      attn_tile<true>(smem + (2 * kq) * REC_B, lane, q0, q1z, mt, o);
      attn_tile<true>(smem + (2 * kq + 1) * REC_B, lane, q0, q1z, mt, o);
    }
  }
}

extern "C" void kernel_launch(void* const* d_in, const int* in_sizes, int n_in, void* d_out, int out_size,
                              void* d_ws, size_t ws_size, hipStream_t stream) {
  const u16* x = (const u16*)d_in[0];
  const u16* wq = (const u16*)d_in[1];
  const u16* wk = (const u16*)d_in[2];
  const u16* wv = (const u16*)d_in[3];
  const u16* wo = (const u16*)d_in[4];
  const u16* w1 = (const u16*)d_in[6];
  const u16* w2 = (const u16*)d_in[7];
  const u16* w3 = (const u16*)d_in[8];
  const u16* w4 = (const u16*)d_in[9];
  u16* out = (u16*)d_out;
  char* ws = (char*)d_ws;
  constexpr size_t ACT = (size_t)S_LEN * DM * 2;
  u16* q = (u16*)(ws + 0 * ACT);
  u16* o = (u16*)(ws + 1 * ACT);
  u16* h = (u16*)(ws + 2 * ACT);
  u16* g1 = (u16*)(ws + 3 * ACT);
  u16* x1 = (u16*)(ws + 4 * ACT);
  u16* G = (u16*)(ws + 5 * ACT);
  char* kvrec = ws + 5 * ACT + (size_t)S_LEN * FF * 2;
  float* kmaxp = (float*)(kvrec + 64 * REC_B + 1024);
  char* wtb = ws + (size_t)24 * 1024 * 1024;
  u16* wo_t = (u16*)wtb;
  u16* w1_t = (u16*)(wtb + (size_t)WT_C640 * 16);
  u16* w2_t = (u16*)(wtb + (size_t)2 * WT_C640 * 16);
  u16* w3_t = (u16*)(wtb + (size_t)3 * WT_C640 * 16);
  u16* w4_t = (u16*)(wtb + (size_t)(3 * WT_C640 + WT_C3) * 16);

  float2* st_h = (float2*)(ws + (size_t)32 * 1024 * 1024);
  float2* st_x1 = st_h + (size_t)S_LEN * 16;
  gemm640_kernel<6, 7, 1, PRO_LN, EPI_QKV><<<256, 512, 0, stream>>>(x, wq, wk, wv, nullptr, q, kvrec, kmaxp, nullptr, nullptr, DM, DM);
  unsigned* cnt = (unsigned*)(ws + (size_t)33 * 1024 * 1024);
  attn_kernel<<<256, 1024, 0, stream>>>(q, kvrec, kmaxp, o, wo, w1, w2, w3, w4, (u32x4*)wtb, cnt);
  post_kernel<<<256, 512, 0, stream>>>(o, x, wtb, h, g1, x1, G, st_h, st_x1, cnt, out);
}
```
